```python
import torch

def _softmax(x, axis=-1):
    return torch.softmax(x, dim=axis)
def _gelu(x):
    return torch.nn.functional.gelu(x, approximate="tanh")
def _silu(x):
    return torch.nn.functional.silu(x)
def _sigmoid(x):
    return torch.sigmoid(x)
def _layernorm(x, axis=-1, eps=1e-5):
    m = x.mean(dim=axis, keepdim=True)
    v = x.var(dim=axis, keepdim=True, unbiased=False)
    return (x - m) / torch.sqrt(v + eps)
def _scatter_add(tgt, idx, src, axis=0):
    return tgt.clone().index_add_(axis, idx, src)
def _segment_sum(src, seg, n):
    out = torch.zeros((n,) + src.shape[1:], dtype=src.dtype)
    return out.index_add_(0, seg, src)
def _pad(x, pw):
    flat = tuple(v for pair in reversed(pw) for v in pair)
    return torch.nn.functional.pad(x, flat)

kernel_name = 'standalone__dense_activation_squared_relu__prompt'

def setup_inputs():

    _w = (lambda a: (lambda t: t.to(torch.bfloat16) if t.is_floating_point() else t)(torch.as_tensor(a)))
    B, S, D, F = 1, 2048, 640, 1728
    return {
        "x": _w((lambda *s: torch.randn(*s, dtype=torch.bfloat16) * 0.05)(B, S, D)),
        "w1": _w((lambda *s: torch.randn(*s, dtype=torch.bfloat16) * 0.05)(D, F)),
        "b1": _w((lambda *s: torch.randn(*s, dtype=torch.bfloat16) * 0.05)(F)),
        "w2": _w((lambda *s: torch.randn(*s, dtype=torch.bfloat16) * 0.05)(F, D)),
        "b2": _w((lambda *s: torch.randn(*s, dtype=torch.bfloat16) * 0.05)(D)),
    }

def reference(x, w1, b1, w2, b2):
    h = torch.einsum("bsd,df->bsf", x, w1) + b1
    zero = torch.zeros_like(h)
    r = torch.where(h > zero, h, zero)
    h = r * r
    out = torch.einsum("bsf,fd->bsd", h, w2) + b2
    return out
```

```cpp
#include <hip/hip_runtime.h>
#include <hip/hip_bf16.h>
#include <stdint.h>

typedef __attribute__((ext_vector_type(8))) short bf16x8;
typedef __attribute__((ext_vector_type(4))) float f32x4;

static __device__ __forceinline__ float bf2f(unsigned short u) { return __uint_as_float(((unsigned)u) << 16); }
static __device__ __forceinline__ unsigned short f2bf(float f) {
    __hip_bfloat16 b = __float2bfloat16(f);
    return *reinterpret_cast<unsigned short*>(&b);
}

template <int EPI>
__global__ __launch_bounds__(64) void gemm_naive(const unsigned short* __restrict__ A, const unsigned short* __restrict__ B,
                                                  const unsigned short* __restrict__ bias, unsigned short* __restrict__ C,
                                                  int M, int N, int K) {
    const int n0 = blockIdx.x * 16, m0 = blockIdx.y * 16;
    const int l = threadIdx.x, r = l & 15, g = l >> 4;
    f32x4 acc = {0.f, 0.f, 0.f, 0.f};
    for (int k0 = 0; k0 < K; k0 += 32) {
        bf16x8 a = *reinterpret_cast<const bf16x8*>(A + (size_t)(m0 + r) * K + k0 + 8 * g);
        bf16x8 b;
#pragma unroll
        for (int j = 0; j < 8; ++j) b[j] = (short)B[(size_t)(k0 + 8 * g + j) * N + n0 + r];
        acc = __builtin_amdgcn_mfma_f32_16x16x32_bf16(a, b, acc, 0, 0, 0);
    }
    const float bv = bf2f(bias[n0 + r]);
#pragma unroll
    for (int j = 0; j < 4; ++j) {
        float v = bf2f(f2bf(acc[j]));
        v = bf2f(f2bf(v + bv));
        if (EPI == 1) {
            v = v > 0.f ? v : 0.f;
            v = v * v;
        }
        C[(size_t)(m0 + 4 * g + j) * N + n0 + r] = f2bf(v);
    }
}

extern "C" void kernel_launch(void* const* d_in, const int* in_sizes, int n_in,
                              void* d_out, int out_size, void* d_ws, size_t ws_size,
                              hipStream_t stream) {
    const unsigned short* x  = (const unsigned short*)d_in[0];
    const unsigned short* w1 = (const unsigned short*)d_in[1];
    const unsigned short* b1 = (const unsigned short*)d_in[2];
    const unsigned short* w2 = (const unsigned short*)d_in[3];
    const unsigned short* b2 = (const unsigned short*)d_in[4];
    unsigned short* out = (unsigned short*)d_out;
    unsigned short* h = (unsigned short*)d_ws;
    const int S = 2048, D = 640, F = 1728;
    gemm_naive<1><<<dim3(F / 16, S / 16), 64, 0, stream>>>(x, w1, b1, h, S, F, D);
    gemm_naive<0><<<dim3(D / 16, S / 16), 64, 0, stream>>>(h, w2, b2, out, S, D, F);
}
```

```cpp
#include <hip/hip_runtime.h>
#include <hip/hip_bf16.h>
#include <stdint.h>

typedef unsigned short u16;
typedef __attribute__((ext_vector_type(8))) short bf16x8;
typedef __attribute__((ext_vector_type(4))) short bf16x4;
typedef __attribute__((ext_vector_type(4))) float f32x4;
typedef __attribute__((ext_vector_type(4))) unsigned u32x4;
typedef __attribute__((ext_vector_type(2))) unsigned u32x2;
#define AS1 __attribute__((address_space(1)))
#define AS3 __attribute__((address_space(3)))

namespace {
constexpr int S_ = 2048, D_ = 640, F_ = 1728;
constexpr int KT_ = 54;
constexpr int HF_ELEMS = S_ * F_;

__device__ __forceinline__ float bf2f(u16 u) { return __uint_as_float(((unsigned)u) << 16); }
__device__ __forceinline__ float bfround(float f) { return (float)(__bf16)f; }
__device__ __forceinline__ unsigned pack2(float lo, float hi) {
    __bf16 a = (__bf16)lo, b = (__bf16)hi;
    return (unsigned)__builtin_bit_cast(u16, a) | ((unsigned)__builtin_bit_cast(u16, b) << 16);
}
__device__ __forceinline__ void dma16(const void* g, unsigned lds_off) {
    asm volatile("s_mov_b32 m0, %1\n\ts_nop 0\n\tglobal_load_lds_dwordx4 %0, off" :: "v"(g), "s"(lds_off) : "memory");
}
#define MFMA16(a, b, c) __builtin_amdgcn_mfma_f32_16x16x32_bf16(a, b, c, 0, 0, 0)

template <int NS>
__global__ __launch_bounds__(256, 1) void k1_gemm1(const u16* __restrict__ x, const u16* __restrict__ w1, const u16* __restrict__ b1,
                                                    const u16* __restrict__ w2, u16* __restrict__ hF, u16* __restrict__ w2F) {
    __shared__ __attribute__((aligned(16))) unsigned char lds[NS * 32768];
    const int id = blockIdx.x, xcd = id & 7, j = id >> 3;
    const int wave = __builtin_amdgcn_readfirstlane(threadIdx.x >> 6), lane = threadIdx.x & 63;
    const int g = lane >> 4;

    if (j >= 28) {
        const int half = xcd & 1;
        const int wi = (xcd >> 1) * 16 + (j - 28) * 4 + wave;
        for (int i0 = 0; i0 < 17; i0 += 4) {
            u16 v[4][8];
#pragma unroll
            for (int u = 0; u < 4; ++u) {
                const int lf = wi + 64 * (i0 + u);
                if (i0 + u < 17 && lf < 1080) {
                    const int fi = half * 1080 + lf;
                    const int db = fi / 270, rem = fi - db * 270, kt = rem / 5, di = rem - kt * 5;
                    const u16* src = w2 + (size_t)(32 * kt + 4 * g) * D_ + 80 * db + 16 * di + (lane & 15);
#pragma unroll
                    for (int jj = 0; jj < 8; ++jj) v[u][jj] = src[(size_t)(16 * (jj >> 2) + (jj & 3)) * D_];
                }
            }
#pragma unroll
            for (int u = 0; u < 4; ++u) {
                const int lf = wi + 64 * (i0 + u);
                if (i0 + u < 17 && lf < 1080) {
                    const int fi = half * 1080 + lf;
                    u32x4 o;
#pragma unroll
                    for (int q = 0; q < 4; ++q) o[q] = (unsigned)v[u][2 * q] | ((unsigned)v[u][2 * q + 1] << 16);
                    *reinterpret_cast<u32x4*>(w2F + (size_t)fi * 512 + lane * 8) = o;
                }
            }
        }
        return;
    }

    const int mt = 4 * (xcd >> 1) + (j & 3);
    const int c = 7 * (xcd & 1) + (j >> 2);
    const int m0 = 128 * mt;
    const int f0 = c == 13 ? (F_ - 128) : 128 * c;
    const int wm = wave >> 1, wf = wave & 1;

    u32x2 bias[4];
#pragma unroll
    for (int t = 0; t < 4; ++t) bias[t] = *reinterpret_cast<const u32x2*>(b1 + f0 + 64 * wf + 16 * t + 4 * g);

    const u16* px = x + (size_t)(m0 + 32 * wave + (lane >> 3)) * D_ + 8 * ((lane & 7) ^ (lane >> 3));
    const int blk0 = ((lane & 15) >> 1) ^ (lane >> 4);
    const u16* pw0 = w1 + (size_t)(16 * wave + (lane >> 4)) * F_ + f0 + 16 * blk0 + 8 * (lane & 1);
    const u16* pw1 = w1 + (size_t)(16 * wave + (lane >> 4)) * F_ + f0 + 16 * (blk0 ^ 4) + 8 * (lane & 1);

    const unsigned ldsbase = (unsigned)(size_t)(AS3 unsigned char*)lds;
    auto issue = [&](int kc) {
        const unsigned sb = ldsbase + (kc % NS) * 32768;
#pragma unroll
        for (int ii = 0; ii < 4; ++ii) dma16(px + (size_t)ii * 8 * D_ + 64 * kc, sb + (4 * wave + ii) * 1024);
#pragma unroll
        for (int ii = 0; ii < 4; ++ii) dma16((ii < 2 ? pw0 : pw1) + (size_t)(64 * kc + 4 * ii) * F_, sb + 16384 + (4 * wave + ii) * 1024);
    };

    const int xrow = (64 * wm + (lane & 15)) * 128;
    const int xoff0 = xrow + ((g ^ (lane & 7)) * 16);
    const int xoff1 = xrow + (((4 + g) ^ (lane & 7)) * 16);
    const int q = (lane >> 2) & 3, p = lane & 3, sw = q | ((g & 1) << 2);
    const int wbase = 16384 + (8 * g + q) * 256 + 8 * p;
    int woff[4];
#pragma unroll
    for (int t = 0; t < 4; ++t) woff[t] = wbase + (((4 * wf + t) ^ sw) << 5);

    f32x4 acc[4][4];
#pragma unroll
    for (int t = 0; t < 4; ++t)
#pragma unroll
        for (int mi = 0; mi < 4; ++mi) acc[t][mi] = (f32x4){0.f, 0.f, 0.f, 0.f};

#pragma unroll
    for (int kc = 0; kc < NS - 1; ++kc) issue(kc);

#pragma unroll
    for (int kc = 0; kc < 10; ++kc) {
        constexpr int dummy = 0; (void)dummy;
        const int ahead = ((kc + NS - 2 < 9) ? (NS - 2) : (9 - kc));
        if (ahead >= 7) asm volatile("s_waitcnt vmcnt(56)" ::: "memory");
        else if (ahead == 6) asm volatile("s_waitcnt vmcnt(48)" ::: "memory");
        else if (ahead == 5) asm volatile("s_waitcnt vmcnt(40)" ::: "memory");
        else if (ahead == 4) asm volatile("s_waitcnt vmcnt(32)" ::: "memory");
        else if (ahead == 3) asm volatile("s_waitcnt vmcnt(24)" ::: "memory");
        else if (ahead == 2) asm volatile("s_waitcnt vmcnt(16)" ::: "memory");
        else if (ahead == 1) asm volatile("s_waitcnt vmcnt(8)" ::: "memory");
        else asm volatile("s_waitcnt vmcnt(0)" ::: "memory");
        __builtin_amdgcn_s_barrier();
        if (kc + NS - 1 < 10) issue(kc + NS - 1);
        const unsigned char* sb = lds + (kc % NS) * 32768;
#pragma unroll
        for (int ks = 0; ks < 2; ++ks) {
            bf16x8 xf[4], wfr[4];
#pragma unroll
            for (int mi = 0; mi < 4; ++mi)
                xf[mi] = *reinterpret_cast<const bf16x8*>(sb + (ks ? xoff1 : xoff0) + mi * 2048);
#pragma unroll
            for (int t = 0; t < 4; ++t) {
                bf16x4 lo = __builtin_amdgcn_ds_read_tr16_b64_v4i16((AS3 bf16x4*)(sb + woff[t] + ks * 8192));
                bf16x4 hi = __builtin_amdgcn_ds_read_tr16_b64_v4i16((AS3 bf16x4*)(sb + woff[t] + ks * 8192 + 1024));
                wfr[t] = __builtin_shufflevector(lo, hi, 0, 1, 2, 3, 4, 5, 6, 7);
            }
#pragma unroll
            for (int t = 0; t < 4; ++t)
#pragma unroll
                for (int mi = 0; mi < 4; ++mi) acc[t][mi] = MFMA16(wfr[t], xf[mi], acc[t][mi]);
        }
        asm volatile("s_waitcnt lgkmcnt(0)" ::: "memory");
    }

    if (c == 13 && wf == 0) return;
    const int mb = 2 * mt + wm;
#pragma unroll
    for (int u = 0; u < 2; ++u) {
        const int kt = (f0 + 64 * wf) / 32 + u;
#pragma unroll
        for (int mi = 0; mi < 4; ++mi) {
            u32x4 o;
#pragma unroll
            for (int hh = 0; hh < 2; ++hh) {
                const int t = 2 * u + hh;
                float r[4];
#pragma unroll
                for (int e = 0; e < 4; ++e) {
                    const unsigned bw = bias[t][e >> 1];
                    const float bv = (e & 1) ? __uint_as_float(bw & 0xffff0000u) : __uint_as_float(bw << 16);
                    float v = bfround(bfround(acc[t][mi][e]) + bv);
                    v = v > 0.f ? v : 0.f;
                    r[e] = v * v;
                }
                o[2 * hh] = pack2(r[0], r[1]);
                o[2 * hh + 1] = pack2(r[2], r[3]);
            }
            *reinterpret_cast<u32x4*>(hF + ((size_t)((mb * KT_ + kt) * 4 + mi) * 64 + lane) * 8) = o;
        }
    }
}

template <int P>
__global__ __launch_bounds__(256, 1) void k2_gemm2(const u16* __restrict__ hF, const u16* __restrict__ w2F, const u16* __restrict__ b2,
                                                    u16* __restrict__ out) {
    __shared__ __attribute__((aligned(16))) f32x4 red[4 * 3 * 5 * 64];
    const int id = blockIdx.x, xcd = id & 7, j = id >> 3;
    const int mb = 8 * (xcd >> 1) + (j & 7), db = 4 * (xcd & 1) + (j >> 3);
    const int wave = __builtin_amdgcn_readfirstlane(threadIdx.x >> 6), lane = threadIdx.x & 63;
    const int g = lane >> 4;
    const int kt0 = wave * 13 + (wave < 2 ? wave : 2);
    const int nk = wave < 2 ? 14 : 13;
    const u16* pa = hF + ((size_t)(mb * KT_ + kt0) * 4) * 512 + lane * 8;
    const u16* pb = w2F + ((size_t)(db * KT_ + kt0) * 5) * 512 + lane * 8;

    u32x2 bias[5];
#pragma unroll
    for (int di = 0; di < 5; ++di) bias[di] = *reinterpret_cast<const u32x2*>(b2 + 80 * db + 16 * di + 4 * g);

    f32x4 acc[5][4];
#pragma unroll
    for (int di = 0; di < 5; ++di)
#pragma unroll
        for (int mi = 0; mi < 4; ++mi) acc[di][mi] = (f32x4){0.f, 0.f, 0.f, 0.f};

    bf16x8 A[P][5], B[P][4];
    auto load = [&](int s, int slot) {
#pragma unroll
        for (int mi = 0; mi < 4; ++mi) B[slot][mi] = *reinterpret_cast<const bf16x8*>(pa + (size_t)s * 2048 + mi * 512);
#pragma unroll
        for (int di = 0; di < 5; ++di) A[slot][di] = *reinterpret_cast<const bf16x8*>(pb + (size_t)s * 2560 + di * 512);
    };
#pragma unroll
    for (int s = 0; s < P; ++s) load(s, s);
#pragma unroll
    for (int s = 0; s < 14; ++s) {
        if (s < 13 || nk == 14) {
#pragma unroll
            for (int di = 0; di < 5; ++di)
#pragma unroll
                for (int mi = 0; mi < 4; ++mi) acc[di][mi] = MFMA16(A[s % P][di], B[s % P][mi], acc[di][mi]);
        }
        if (s + P < 13 || (s + P == 13 && nk == 14)) load(s + P, s % P);
        __builtin_amdgcn_sched_barrier(0);
    }

#pragma unroll
    for (int dw = 0; dw < 4; ++dw) {
        if (dw != wave) {
            const int slot = wave < dw ? wave : wave - 1;
#pragma unroll
            for (int di = 0; di < 5; ++di) red[((dw * 3 + slot) * 5 + di) * 64 + lane] = acc[di][dw];
        }
    }
    __syncthreads();
    f32x4 fin[5];
#pragma unroll
    for (int di = 0; di < 5; ++di) {
        fin[di] = wave == 0 ? acc[di][0] : wave == 1 ? acc[di][1] : wave == 2 ? acc[di][2] : acc[di][3];
#pragma unroll
        for (int sl = 0; sl < 3; ++sl) fin[di] += red[((wave * 3 + sl) * 5 + di) * 64 + lane];
    }
    const int m = 64 * mb + 16 * wave + (lane & 15);
#pragma unroll
    for (int di = 0; di < 5; ++di) {
        float r[4];
#pragma unroll
        for (int e = 0; e < 4; ++e) {
            const unsigned bw = bias[di][e >> 1];
            const float bv = (e & 1) ? __uint_as_float(bw & 0xffff0000u) : __uint_as_float(bw << 16);
            r[e] = bfround(fin[di][e]) + bv;
        }
        u32x2 o = {pack2(r[0], r[1]), pack2(r[2], r[3])};
        *reinterpret_cast<u32x2*>(out + (size_t)m * D_ + 80 * db + 16 * di + 4 * g) = o;
    }
}
}

extern "C" void kernel_launch(void* const* d_in, const int* in_sizes, int n_in, void* d_out, int out_size, void* d_ws, size_t ws_size,
                              hipStream_t stream) {
    const u16* x = (const u16*)d_in[0];
    const u16* w1 = (const u16*)d_in[1];
    const u16* b1 = (const u16*)d_in[2];
    const u16* w2 = (const u16*)d_in[3];
    const u16* b2 = (const u16*)d_in[4];
    u16* hF = (u16*)d_ws;
    u16* w2F = hF + (size_t)HF_ELEMS;
    k1_gemm1<4><<<256, 256, 0, stream>>>(x, w1, b1, w2, hF, w2F);
    k2_gemm2<6><<<256, 256, 0, stream>>>(hF, w2F, b2, (u16*)d_out);
}
```

```cpp
#include <hip/hip_runtime.h>
#include <hip/hip_bf16.h>
#include <stdint.h>

typedef unsigned short u16;
typedef __attribute__((ext_vector_type(8))) short bf16x8;
typedef __attribute__((ext_vector_type(4))) short bf16x4;
typedef __attribute__((ext_vector_type(4))) float f32x4;
typedef __attribute__((ext_vector_type(4))) unsigned u32x4;
typedef __attribute__((ext_vector_type(2))) unsigned u32x2;
#define TSARG
#define TSPASS(p)
#define TS(i)
#define AS1 __attribute__((address_space(1)))
#define AS3 __attribute__((address_space(3)))

#define K1_NS 4
#define K1_NL 2
namespace {
constexpr int S_ = 2048, D_ = 640, F_ = 1728;
constexpr int KT_ = 54;
constexpr int HF_ELEMS = S_ * F_;

__device__ __forceinline__ float bf2f(u16 u) { return __uint_as_float(((unsigned)u) << 16); }
__device__ __forceinline__ float bfround(float f) { return (float)(__bf16)f; }
__device__ __forceinline__ unsigned pack2(float lo, float hi) {
    __bf16 a = (__bf16)lo, b = (__bf16)hi;
    return (unsigned)__builtin_bit_cast(u16, a) | ((unsigned)__builtin_bit_cast(u16, b) << 16);
}
__device__ __forceinline__ void dma16(const void* g, unsigned lds_off) {
    asm volatile("s_mov_b32 m0, %1\n\ts_nop 0\n\tglobal_load_lds_dwordx4 %0, off" :: "v"(g), "s"(lds_off) : "memory");
}
typedef __attribute__((ext_vector_type(2))) float f32x2;
typedef __attribute__((ext_vector_type(2))) __bf16 bf16x2_t;
__device__ __forceinline__ unsigned cvtpk(f32x2 v) { return __builtin_bit_cast(unsigned, __builtin_convertvector(v, bf16x2_t)); }
__device__ __forceinline__ f32x2 unpk(unsigned u) { return (f32x2){__uint_as_float(u << 16), __uint_as_float(u & 0xffff0000u)}; }
__device__ __forceinline__ unsigned epi1(f32x2 a, f32x2 b) {
    f32x2 y = unpk(cvtpk(a)) + b;
    y = unpk(cvtpk(y));
    y = __builtin_elementwise_max(y, (f32x2){0.f, 0.f});
    return cvtpk(y * y);
}
__device__ __forceinline__ unsigned epi2(f32x2 a, f32x2 b) { return cvtpk(unpk(cvtpk(a)) + b); }
#define MFMA16(a, b, c) __builtin_amdgcn_mfma_f32_16x16x32_bf16(a, b, c, 0, 0, 0)

template <int NS>
__global__ __launch_bounds__(384, 1) void k1_gemm1(const u16* __restrict__ x, const u16* __restrict__ w1, const u16* __restrict__ b1,
                                                    const u16* __restrict__ w2, u16* __restrict__ hF, u16* __restrict__ w2F TSARG) {
    TS(0);
    __shared__ __attribute__((aligned(16))) unsigned char lds[NS * 32768];
    const int id = blockIdx.x, xcd = id & 7, j = id >> 3;
    const int wave = __builtin_amdgcn_readfirstlane(threadIdx.x >> 6), lane = threadIdx.x & 63;
    const int g = lane >> 4;

    if (j >= 28) {
        if (wave >= 4) return;
        const int half = xcd & 1;
        const int wi = (xcd >> 1) * 16 + (j - 28) * 4 + wave;
        u16 v[17][8];
#pragma unroll
        for (int u = 0; u < 17; ++u) {
            const int lf = wi + 64 * u;
            const int fi = half * 1080 + (lf < 1080 ? lf : 1079);
            const int db = fi / 270, rem = fi - db * 270, kt = rem / 5, di = rem - kt * 5;
            const u16* src = w2 + (size_t)(32 * kt + 4 * g) * D_ + 80 * db + 16 * di + (lane & 15);
#pragma unroll
            for (int jj = 0; jj < 8; ++jj) v[u][jj] = src[(size_t)(16 * (jj >> 2) + (jj & 3)) * D_];
        }
#pragma unroll
        for (int u = 0; u < 17; ++u) {
            const int lf = wi + 64 * u;
            if (lf < 1080) {
                const int fi = half * 1080 + lf;
                u32x4 o;
#pragma unroll
                for (int q = 0; q < 4; ++q) o[q] = (unsigned)v[u][2 * q] | ((unsigned)v[u][2 * q + 1] << 16);
                *reinterpret_cast<u32x4*>(w2F + (size_t)fi * 512 + lane * 8) = o;
            }
        }
        TS(1); TS(2);
        return;
    }

    const int mt = 4 * (xcd >> 1) + (j & 3);
    const int c = 7 * (xcd & 1) + (j >> 2);
    const int m0 = 128 * mt;
    const int f0 = c == 13 ? (F_ - 128) : 128 * c;

    if (wave >= 4) {
        const unsigned ldsbase = (unsigned)(size_t)(AS3 unsigned char*)lds;
        auto run_loader = [&](auto issue) {
#pragma unroll
            for (int kc = 0; kc < NS - 1; ++kc) issue(kc);
#pragma unroll
            for (int kc = 0; kc < 10; ++kc) {
                const int ahead = ((kc + NS - 2 < 9) ? (NS - 2) : (9 - kc));
                if (ahead >= 4) asm volatile("s_waitcnt vmcnt(63)" ::: "memory");
                else if (ahead == 3) asm volatile("s_waitcnt vmcnt(48)" ::: "memory");
                else if (ahead == 2) asm volatile("s_waitcnt vmcnt(32)" ::: "memory");
                else if (ahead == 1) asm volatile("s_waitcnt vmcnt(16)" ::: "memory");
                else asm volatile("s_waitcnt vmcnt(0)" ::: "memory");
                __builtin_amdgcn_s_barrier();
                if (kc + NS - 1 < 10) issue(kc + NS - 1);
            }
        };
        if (wave == 4) {
            const u16* px = x + (size_t)(m0 + (lane >> 3)) * D_ + 8 * ((lane & 7) ^ (lane >> 3));
            run_loader([&](int kc) {
                const unsigned sb = ldsbase + (kc % NS) * 32768;
#pragma unroll
                for (int i = 0; i < 16; ++i) dma16(px + (size_t)i * 8 * D_ + 64 * kc, sb + i * 1024);
            });
        } else {
            const int blk0 = ((lane & 15) >> 1) ^ (lane >> 4);
            const u16* pw0 = w1 + (size_t)(lane >> 4) * F_ + f0 + 16 * blk0 + 8 * (lane & 1);
            const u16* pw1 = w1 + (size_t)(lane >> 4) * F_ + f0 + 16 * (blk0 ^ 4) + 8 * (lane & 1);
            run_loader([&](int kc) {
                const unsigned sb = ldsbase + (kc % NS) * 32768;
#pragma unroll
                for (int i = 0; i < 16; ++i) dma16(((i & 2) ? pw1 : pw0) + (size_t)(64 * kc + 4 * i) * F_, sb + 16384 + i * 1024);
            });
        }
        TS(1); TS(2);
        return;
    }

    const int wm = wave >> 1, wf = wave & 1;
    u32x2 bias[4];
#pragma unroll
    for (int t = 0; t < 4; ++t) bias[t] = *reinterpret_cast<const u32x2*>(b1 + f0 + 64 * wf + 16 * t + 4 * g);
#pragma unroll
    for (int t = 0; t < 4; ++t) asm volatile("" : "+v"(bias[t]));

    const int xrow = (64 * wm + (lane & 15)) * 128;
    const int xoff0 = xrow + ((g ^ (lane & 7)) * 16);
    const int xoff1 = xrow + (((4 + g) ^ (lane & 7)) * 16);
    const int q = (lane >> 2) & 3, p = lane & 3, sw = q | ((g & 1) << 2);
    const int wbase = 16384 + (8 * g + q) * 256 + 8 * p;
    int woff[4];
#pragma unroll
    for (int t = 0; t < 4; ++t) woff[t] = wbase + (((4 * wf + t) ^ sw) << 5);

    f32x4 acc[4][4];
#pragma unroll
    for (int t = 0; t < 4; ++t)
#pragma unroll
        for (int mi = 0; mi < 4; ++mi) acc[t][mi] = (f32x4){0.f, 0.f, 0.f, 0.f};

    bf16x8 xf[2][2][4], wfr[2][2][4];
#pragma unroll
    for (int kc = 0; kc <= 10; ++kc) {
        if (kc < 10) {
            __builtin_amdgcn_s_barrier();
            if (kc == 9) TS(3);
            const unsigned char* sb = lds + (kc % NS) * 32768;
#pragma unroll
            for (int ks = 0; ks < 2; ++ks) {
#pragma unroll
                for (int mi = 0; mi < 4; ++mi)
                    xf[kc & 1][ks][mi] = *reinterpret_cast<const bf16x8*>(sb + (ks ? xoff1 : xoff0) + mi * 2048);
#pragma unroll
                for (int t = 0; t < 4; ++t) {
                    bf16x4 lo = __builtin_amdgcn_ds_read_tr16_b64_v4i16((AS3 bf16x4*)(sb + woff[t] + ks * 8192));
                    bf16x4 hi = __builtin_amdgcn_ds_read_tr16_b64_v4i16((AS3 bf16x4*)(sb + woff[t] + ks * 8192 + 1024));
                    wfr[kc & 1][ks][t] = __builtin_shufflevector(lo, hi, 0, 1, 2, 3, 4, 5, 6, 7);
                }
            }
        }
        __builtin_amdgcn_sched_barrier(0);
        if (kc > 0) {
#pragma unroll
            for (int ks = 0; ks < 2; ++ks)
#pragma unroll
                for (int t = 0; t < 4; ++t)
#pragma unroll
                    for (int mi = 0; mi < 4; ++mi) acc[t][mi] = MFMA16(wfr[(kc - 1) & 1][ks][t], xf[(kc - 1) & 1][ks][mi], acc[t][mi]);
        }
        __builtin_amdgcn_sched_barrier(0);
        if (kc < 10) asm volatile("s_waitcnt lgkmcnt(0)" ::: "memory");
    }

    TS(1);
    if (c == 13 && wf == 0) { TS(2); return; }
    const int mb = 2 * mt + wm;
#pragma unroll
    for (int u = 0; u < 2; ++u) {
        const int kt = (f0 + 64 * wf) / 32 + u;
#pragma unroll
        for (int mi = 0; mi < 4; ++mi) {
            u32x4 o;
#pragma unroll
            for (int hh = 0; hh < 2; ++hh) {
                const int t = 2 * u + hh;
                o[2 * hh] = epi1((f32x2){acc[t][mi][0], acc[t][mi][1]}, unpk(bias[t][0]));
                o[2 * hh + 1] = epi1((f32x2){acc[t][mi][2], acc[t][mi][3]}, unpk(bias[t][1]));
            }
            *reinterpret_cast<u32x4*>(hF + ((size_t)((mb * KT_ + kt) * 4 + mi) * 64 + lane) * 8) = o;
        }
    }
    TS(2);
}

template <int P>
__global__ __launch_bounds__(256, 1) void k2_gemm2(const u16* __restrict__ hF, const u16* __restrict__ w2F, const u16* __restrict__ b2,
                                                    u16* __restrict__ out TSARG) {
    TS(0);
    __shared__ __attribute__((aligned(16))) f32x4 red[4 * 4 * 5 * 64];
    const int id = blockIdx.x, xcd = id & 7, j = id >> 3;
    const int mb = 8 * (xcd >> 1) + (j & 7), db = 4 * (xcd & 1) + (j >> 3);
    const int wave = __builtin_amdgcn_readfirstlane(threadIdx.x >> 6), lane = threadIdx.x & 63;
    const int g = lane >> 4;
    const int kt0 = wave * 13 + (wave < 2 ? wave : 2);
    const int nk = wave < 2 ? 14 : 13;
    const u16* pa = hF + ((size_t)(mb * KT_ + kt0) * 4) * 512 + lane * 8;
    const u16* pb = w2F + ((size_t)(db * KT_ + kt0) * 5) * 512 + lane * 8;

    u32x2 bias[5];
#pragma unroll
    for (int di = 0; di < 5; ++di) bias[di] = *reinterpret_cast<const u32x2*>(b2 + 80 * db + 16 * di + 4 * g);

    f32x4 acc[5][4];
#pragma unroll
    for (int di = 0; di < 5; ++di)
#pragma unroll
        for (int mi = 0; mi < 4; ++mi) acc[di][mi] = (f32x4){0.f, 0.f, 0.f, 0.f};

    bf16x8 A[P][5], B[P][4];
    auto load = [&](int s, int slot) {
#pragma unroll
        for (int mi = 0; mi < 4; ++mi) B[slot][mi] = *reinterpret_cast<const bf16x8*>(pa + (size_t)s * 2048 + mi * 512);
#pragma unroll
        for (int di = 0; di < 5; ++di) A[slot][di] = *reinterpret_cast<const bf16x8*>(pb + (size_t)s * 2560 + di * 512);
    };
#pragma unroll
    for (int s = 0; s < P; ++s) load(s, s);
    __builtin_amdgcn_sched_barrier(0);
#pragma unroll
    for (int di = 0; di < 5; ++di) asm volatile("" : "+v"(bias[di]));
#pragma unroll
    for (int s = 0; s < 14; ++s) {
        if (s < 13 || nk == 14) {
#pragma unroll
            for (int di = 0; di < 5; ++di)
#pragma unroll
                for (int mi = 0; mi < 4; ++mi) acc[di][mi] = MFMA16(A[s % P][di], B[s % P][mi], acc[di][mi]);
        }
        if (s + P < 13 || (s + P == 13 && nk == 14)) load(s + P, s % P);
        __builtin_amdgcn_sched_barrier(0);
    }

    TS(1);
#pragma unroll
    for (int di = 0; di < 5; ++di)
#pragma unroll
        for (int mi = 0; mi < 4; ++mi) red[((wave * 4 + mi) * 5 + di) * 64 + lane] = acc[di][mi];
    TS(3);
    __syncthreads();
    TS(4);
    f32x4 fin[5];
#pragma unroll
    for (int di = 0; di < 5; ++di) {
        fin[di] = red[((0 * 4 + wave) * 5 + di) * 64 + lane];
#pragma unroll
        for (int sw_ = 1; sw_ < 4; ++sw_) fin[di] += red[((sw_ * 4 + wave) * 5 + di) * 64 + lane];
    }
    for (int di = 0; di < 5; ++di) asm volatile("" :: "v"(fin[di]));
    TS(5);
    const int m = 64 * mb + 16 * wave + (lane & 15);
#pragma unroll
    for (int di = 0; di < 5; ++di) {
        u32x2 o = {epi2((f32x2){fin[di][0], fin[di][1]}, unpk(bias[di][0])), epi2((f32x2){fin[di][2], fin[di][3]}, unpk(bias[di][1]))};
        *reinterpret_cast<u32x2*>(out + (size_t)m * D_ + 80 * db + 16 * di + 4 * g) = o;
    }
    TS(2);
}
}

extern "C" void kernel_launch(void* const* d_in, const int* in_sizes, int n_in, void* d_out, int out_size, void* d_ws, size_t ws_size,
                              hipStream_t stream) {
    const u16* x = (const u16*)d_in[0];
    const u16* w1 = (const u16*)d_in[1];
    const u16* b1 = (const u16*)d_in[2];
    const u16* w2 = (const u16*)d_in[3];
    const u16* b2 = (const u16*)d_in[4];
    u16* hF = (u16*)d_ws;
    u16* w2F = hF + (size_t)HF_ELEMS;
    k1_gemm1<K1_NS><<<256, 384, 0, stream>>>(x, w1, b1, w2, hF, w2F TSPASS(ts));
    k2_gemm2<6><<<256, 256, 0, stream>>>(hF, w2F, b2, (u16*)d_out TSPASS(ts + 8 * 4096));
}
```

```cpp
#include <hip/hip_runtime.h>
#include <hip/hip_bf16.h>
#include <stdint.h>

typedef unsigned short u16;
typedef __attribute__((ext_vector_type(8))) short bf16x8;
typedef __attribute__((ext_vector_type(4))) short bf16x4;
typedef __attribute__((ext_vector_type(4))) float f32x4;
typedef __attribute__((ext_vector_type(4))) unsigned u32x4;
typedef __attribute__((ext_vector_type(2))) unsigned u32x2;
#define TSARG
#define TSPASS(p)
#define TS(i)
#define AS1 __attribute__((address_space(1)))
#define AS3 __attribute__((address_space(3)))

#define K1_NS 4
#define K1_TOUCH 1
namespace {
constexpr int S_ = 2048, D_ = 640, F_ = 1728;
constexpr int KT_ = 54;
constexpr int HF_ELEMS = S_ * F_;

__device__ __forceinline__ float bf2f(u16 u) { return __uint_as_float(((unsigned)u) << 16); }
__device__ __forceinline__ float bfround(float f) { return (float)(__bf16)f; }
__device__ __forceinline__ unsigned pack2(float lo, float hi) {
    __bf16 a = (__bf16)lo, b = (__bf16)hi;
    return (unsigned)__builtin_bit_cast(u16, a) | ((unsigned)__builtin_bit_cast(u16, b) << 16);
}
__device__ __forceinline__ void dma16(const void* g, unsigned lds_off) {
    asm volatile("s_mov_b32 m0, %1\n\ts_nop 0\n\tglobal_load_lds_dwordx4 %0, off" :: "v"(g), "s"(lds_off) : "memory");
}
typedef __attribute__((ext_vector_type(2))) float f32x2;
typedef __attribute__((ext_vector_type(2))) __bf16 bf16x2_t;
__device__ __forceinline__ unsigned cvtpk(f32x2 v) { return __builtin_bit_cast(unsigned, __builtin_convertvector(v, bf16x2_t)); }
__device__ __forceinline__ f32x2 unpk(unsigned u) { return (f32x2){__uint_as_float(u << 16), __uint_as_float(u & 0xffff0000u)}; }
__device__ __forceinline__ unsigned epi1(f32x2 a, f32x2 b) {
    f32x2 y = unpk(cvtpk(a)) + b;
    y = unpk(cvtpk(y));
    y = __builtin_elementwise_max(y, (f32x2){0.f, 0.f});
    return cvtpk(y * y);
}
__device__ __forceinline__ unsigned epi2(f32x2 a, f32x2 b) { return cvtpk(unpk(cvtpk(a)) + b); }
#define MFMA16(a, b, c) __builtin_amdgcn_mfma_f32_16x16x32_bf16(a, b, c, 0, 0, 0)

template <int NS>
__global__ __launch_bounds__(384, 1) void k1_gemm1(const u16* __restrict__ x, const u16* __restrict__ w1, const u16* __restrict__ b1,
                                                    const u16* __restrict__ w2, u16* __restrict__ hF, u16* __restrict__ w2F TSARG) {
    TS(0);
    __shared__ __attribute__((aligned(16))) unsigned char lds[NS * 32768];
    const int id = blockIdx.x, xcd = id & 7, j = id >> 3;
    const int wave = __builtin_amdgcn_readfirstlane(threadIdx.x >> 6), lane = threadIdx.x & 63;
    const int g = lane >> 4;

    if (j >= 28) {
        if (wave >= 4) return;
        const int half = xcd & 1;
        const int wi = (xcd >> 1) * 16 + (j - 28) * 4 + wave;
        u16 v[17][8];
#pragma unroll
        for (int u = 0; u < 17; ++u) {
            const int lf = wi + 64 * u;
            const int fi = half * 1080 + (lf < 1080 ? lf : 1079);
            const int db = fi / 270, rem = fi - db * 270, kt = rem / 5, di = rem - kt * 5;
            const u16* src = w2 + (size_t)(32 * kt + 4 * g) * D_ + 80 * db + 16 * di + (lane & 15);
#pragma unroll
            for (int jj = 0; jj < 8; ++jj) v[u][jj] = src[(size_t)(16 * (jj >> 2) + (jj & 3)) * D_];
        }
#pragma unroll
        for (int u = 0; u < 17; ++u) {
            const int lf = wi + 64 * u;
            if (lf < 1080) {
                const int fi = half * 1080 + lf;
                u32x4 o;
#pragma unroll
                for (int q = 0; q < 4; ++q) o[q] = (unsigned)v[u][2 * q] | ((unsigned)v[u][2 * q + 1] << 16);
                *reinterpret_cast<u32x4*>(w2F + (size_t)fi * 512 + lane * 8) = o;
            }
        }
        TS(1); TS(2);
        return;
    }

    const int mt = 4 * (xcd >> 1) + (j & 3);
    const int c = 7 * (xcd & 1) + (j >> 2);
    const int m0 = 128 * mt;
    const int f0 = c == 13 ? (F_ - 128) : 128 * c;

    if (wave >= 4) {
        const unsigned ldsbase = (unsigned)(size_t)(AS3 unsigned char*)lds;
        auto run_loader = [&](auto issue) {
#pragma unroll
            for (int kc = 0; kc < NS - 1; ++kc) issue(kc);
#pragma unroll
            for (int kc = 0; kc < 10; ++kc) {
                const int ahead = ((kc + NS - 2 < 9) ? (NS - 2) : (9 - kc));
                if (ahead >= 4) asm volatile("s_waitcnt vmcnt(63)" ::: "memory");
                else if (ahead == 3) asm volatile("s_waitcnt vmcnt(48)" ::: "memory");
                else if (ahead == 2) asm volatile("s_waitcnt vmcnt(32)" ::: "memory");
                else if (ahead == 1) asm volatile("s_waitcnt vmcnt(16)" ::: "memory");
                else asm volatile("s_waitcnt vmcnt(0)" ::: "memory");
                __builtin_amdgcn_s_barrier();
                if (kc + NS - 1 < 10) issue(kc + NS - 1);
            }
        };
        if (wave == 4) {
            const u16* px = x + (size_t)(m0 + (lane >> 3)) * D_ + 8 * ((lane & 7) ^ (lane >> 3));
            run_loader([&](int kc) {
                const unsigned sb = ldsbase + (kc % NS) * 32768;
#pragma unroll
                for (int i = 0; i < 16; ++i) dma16(px + (size_t)i * 8 * D_ + 64 * kc, sb + i * 1024);
            });
        } else {
            const int blk0 = ((lane & 15) >> 1) ^ (lane >> 4);
            const u16* pw0 = w1 + (size_t)(lane >> 4) * F_ + f0 + 16 * blk0 + 8 * (lane & 1);
            const u16* pw1 = w1 + (size_t)(lane >> 4) * F_ + f0 + 16 * (blk0 ^ 4) + 8 * (lane & 1);
            run_loader([&](int kc) {
                const unsigned sb = ldsbase + (kc % NS) * 32768;
#pragma unroll
                for (int i = 0; i < 16; ++i) dma16(((i & 2) ? pw1 : pw0) + (size_t)(64 * kc + 4 * i) * F_, sb + 16384 + i * 1024);
            });
        }
        TS(1); TS(2);
        return;
    }

    const int wm = wave >> 1, wf = wave & 1;
    unsigned tch[2];
#pragma unroll
    for (int i = 0; i < 2; ++i) {
        const int L = (j * 4 + wave) * 128 + i * 64 + lane;
        const u16* a;
        if (L < 5120) a = x + (size_t)(512 * (xcd >> 1) + L / 10) * D_ + (L % 10) * 64;
        else {
            const int L2 = L - 5120 < 8960 ? L - 5120 : 0;
            const int col = 896 * (xcd & 1) + (L2 % 14) * 64;
            a = w1 + (size_t)(L2 / 14) * F_ + (col < F_ ? col : 0);
        }
        tch[i] = *reinterpret_cast<const unsigned*>(a);
    }
    u32x2 bias[4];
#pragma unroll
    for (int t = 0; t < 4; ++t) bias[t] = *reinterpret_cast<const u32x2*>(b1 + f0 + 64 * wf + 16 * t + 4 * g);
#pragma unroll
    for (int t = 0; t < 4; ++t) asm volatile("" : "+v"(bias[t]));

    const int xrow = (64 * wm + (lane & 15)) * 128;
    const int xoff0 = xrow + ((g ^ (lane & 7)) * 16);
    const int xoff1 = xrow + (((4 + g) ^ (lane & 7)) * 16);
    const int q = (lane >> 2) & 3, p = lane & 3, sw = q | ((g & 1) << 2);
    const int wbase = 16384 + (8 * g + q) * 256 + 8 * p;
    int woff[4];
#pragma unroll
    for (int t = 0; t < 4; ++t) woff[t] = wbase + (((4 * wf + t) ^ sw) << 5);

    f32x4 acc[4][4];
#pragma unroll
    for (int t = 0; t < 4; ++t)
#pragma unroll
        for (int mi = 0; mi < 4; ++mi) acc[t][mi] = (f32x4){0.f, 0.f, 0.f, 0.f};

    bf16x8 xf[2][2][4], wfr[2][2][4];
#pragma unroll
    for (int kc = 0; kc <= 10; ++kc) {
        if (kc < 10) {
            __builtin_amdgcn_s_barrier();
            if (kc == 9) TS(3);
            const unsigned char* sb = lds + (kc % NS) * 32768;
#pragma unroll
            for (int ks = 0; ks < 2; ++ks) {
#pragma unroll
                for (int mi = 0; mi < 4; ++mi)
                    xf[kc & 1][ks][mi] = *reinterpret_cast<const bf16x8*>(sb + (ks ? xoff1 : xoff0) + mi * 2048);
#pragma unroll
                for (int t = 0; t < 4; ++t) {
                    bf16x4 lo = __builtin_amdgcn_ds_read_tr16_b64_v4i16((AS3 bf16x4*)(sb + woff[t] + ks * 8192));
                    bf16x4 hi = __builtin_amdgcn_ds_read_tr16_b64_v4i16((AS3 bf16x4*)(sb + woff[t] + ks * 8192 + 1024));
                    wfr[kc & 1][ks][t] = __builtin_shufflevector(lo, hi, 0, 1, 2, 3, 4, 5, 6, 7);
                }
            }
        }
        __builtin_amdgcn_sched_barrier(0);
        if (kc > 0) {
#pragma unroll
            for (int ks = 0; ks < 2; ++ks)
#pragma unroll
                for (int t = 0; t < 4; ++t)
#pragma unroll
                    for (int mi = 0; mi < 4; ++mi) acc[t][mi] = MFMA16(wfr[(kc - 1) & 1][ks][t], xf[(kc - 1) & 1][ks][mi], acc[t][mi]);
        }
        __builtin_amdgcn_sched_barrier(0);
        if (kc < 10) asm volatile("s_waitcnt lgkmcnt(0)" ::: "memory");
    }

    asm volatile("" :: "v"(tch[0]), "v"(tch[1]));
    TS(1);
    if (c == 13 && wf == 0) { TS(2); return; }
    const int mb = 2 * mt + wm;
#pragma unroll
    for (int u = 0; u < 2; ++u) {
        const int kt = (f0 + 64 * wf) / 32 + u;
#pragma unroll
        for (int mi = 0; mi < 4; ++mi) {
            u32x4 o;
#pragma unroll
            for (int hh = 0; hh < 2; ++hh) {
                const int t = 2 * u + hh;
                o[2 * hh] = epi1((f32x2){acc[t][mi][0], acc[t][mi][1]}, unpk(bias[t][0]));
                o[2 * hh + 1] = epi1((f32x2){acc[t][mi][2], acc[t][mi][3]}, unpk(bias[t][1]));
            }
            *reinterpret_cast<u32x4*>(hF + ((size_t)((mb * KT_ + kt) * 4 + mi) * 64 + lane) * 8) = o;
        }
    }
    TS(2);
}

template <int P>
__global__ __launch_bounds__(256, 1) void k2_gemm2(const u16* __restrict__ hF, const u16* __restrict__ w2F, const u16* __restrict__ b2,
                                                    u16* __restrict__ out TSARG) {
    TS(0);
    __shared__ __attribute__((aligned(16))) f32x4 red[4 * 4 * 5 * 64];
    const int id = blockIdx.x, xcd = id & 7, j = id >> 3;
    const int mb = 8 * (xcd >> 1) + (j & 7), db = 4 * (xcd & 1) + (j >> 3);
    const int wave = __builtin_amdgcn_readfirstlane(threadIdx.x >> 6), lane = threadIdx.x & 63;
    const int g = lane >> 4;
    const int kt0 = wave * 13 + (wave < 2 ? wave : 2);
    const int nk = wave < 2 ? 14 : 13;
    const u16* pa = hF + ((size_t)(mb * KT_ + kt0) * 4) * 512 + lane * 8;
    const u16* pb = w2F + ((size_t)(db * KT_ + kt0) * 5) * 512 + lane * 8;

    u32x2 bias[5];
#pragma unroll
    for (int di = 0; di < 5; ++di) bias[di] = *reinterpret_cast<const u32x2*>(b2 + 80 * db + 16 * di + 4 * g);

    f32x4 acc[5][4];
#pragma unroll
    for (int di = 0; di < 5; ++di)
#pragma unroll
        for (int mi = 0; mi < 4; ++mi) acc[di][mi] = (f32x4){0.f, 0.f, 0.f, 0.f};

    bf16x8 A[P][5], B[P][4];
    auto load = [&](int s, int slot) {
#pragma unroll
        for (int mi = 0; mi < 4; ++mi) B[slot][mi] = *reinterpret_cast<const bf16x8*>(pa + (size_t)s * 2048 + mi * 512);
#pragma unroll
        for (int di = 0; di < 5; ++di) A[slot][di] = *reinterpret_cast<const bf16x8*>(pb + (size_t)s * 2560 + di * 512);
    };
#pragma unroll
    for (int s = 0; s < P; ++s) load(s, s);
    __builtin_amdgcn_sched_barrier(0);
#pragma unroll
    for (int di = 0; di < 5; ++di) asm volatile("" : "+v"(bias[di]));
#pragma unroll
    for (int s = 0; s < 14; ++s) {
        if (s < 13 || nk == 14) {
#pragma unroll
            for (int di = 0; di < 5; ++di)
#pragma unroll
                for (int mi = 0; mi < 4; ++mi) acc[di][mi] = MFMA16(A[s % P][di], B[s % P][mi], acc[di][mi]);
        }
        if (s + P < 13 || (s + P == 13 && nk == 14)) load(s + P, s % P);
        __builtin_amdgcn_sched_barrier(0);
    }

    TS(1);
#pragma unroll
    for (int di = 0; di < 5; ++di)
#pragma unroll
        for (int mi = 0; mi < 4; ++mi) red[((wave * 4 + mi) * 5 + di) * 64 + lane] = acc[di][mi];
    TS(3);
    __syncthreads();
    TS(4);
    f32x4 fin[5];
#pragma unroll
    for (int di = 0; di < 5; ++di) {
        fin[di] = red[((0 * 4 + wave) * 5 + di) * 64 + lane];
#pragma unroll
        for (int sw_ = 1; sw_ < 4; ++sw_) fin[di] += red[((sw_ * 4 + wave) * 5 + di) * 64 + lane];
    }
    for (int di = 0; di < 5; ++di) asm volatile("" :: "v"(fin[di]));
    TS(5);
    const int m = 64 * mb + 16 * wave + (lane & 15);
#pragma unroll
    for (int di = 0; di < 5; ++di) {
        u32x2 o = {epi2((f32x2){fin[di][0], fin[di][1]}, unpk(bias[di][0])), epi2((f32x2){fin[di][2], fin[di][3]}, unpk(bias[di][1]))};
        *reinterpret_cast<u32x2*>(out + (size_t)m * D_ + 80 * db + 16 * di + 4 * g) = o;
    }
    TS(2);
}
}

extern "C" void kernel_launch(void* const* d_in, const int* in_sizes, int n_in, void* d_out, int out_size, void* d_ws, size_t ws_size,
                              hipStream_t stream) {
    const u16* x = (const u16*)d_in[0];
    const u16* w1 = (const u16*)d_in[1];
    const u16* b1 = (const u16*)d_in[2];
    const u16* w2 = (const u16*)d_in[3];
    const u16* b2 = (const u16*)d_in[4];
    u16* hF = (u16*)d_ws;
    u16* w2F = hF + (size_t)HF_ELEMS;
    k1_gemm1<K1_NS><<<256, 384, 0, stream>>>(x, w1, b1, w2, hF, w2F TSPASS(ts));
    k2_gemm2<6><<<256, 256, 0, stream>>>(hF, w2F, b2, (u16*)d_out TSPASS(ts + 8 * 4096));
}
```

```cpp
#include <hip/hip_runtime.h>
#include <hip/hip_bf16.h>
#include <stdint.h>

typedef unsigned short u16;
typedef __attribute__((ext_vector_type(8))) short bf16x8;
typedef __attribute__((ext_vector_type(4))) short bf16x4;
typedef __attribute__((ext_vector_type(4))) float f32x4;
typedef __attribute__((ext_vector_type(4))) unsigned u32x4;
typedef __attribute__((ext_vector_type(2))) unsigned u32x2;
#define TSARG
#define TSPASS(p)
#define TS(i)
#define AS1 __attribute__((address_space(1)))
#define AS3 __attribute__((address_space(3)))

#define K1_NS 4
#define K1_TOUCH 2
#define K2_TOUCH 0
namespace {
constexpr int S_ = 2048, D_ = 640, F_ = 1728;
constexpr int KT_ = 54;
constexpr int HF_ELEMS = S_ * F_;

__device__ __forceinline__ float bf2f(u16 u) { return __uint_as_float(((unsigned)u) << 16); }
__device__ __forceinline__ float bfround(float f) { return (float)(__bf16)f; }
__device__ __forceinline__ unsigned pack2(float lo, float hi) {
    __bf16 a = (__bf16)lo, b = (__bf16)hi;
    return (unsigned)__builtin_bit_cast(u16, a) | ((unsigned)__builtin_bit_cast(u16, b) << 16);
}
__device__ __forceinline__ void dma16(const void* g, unsigned lds_off) {
    asm volatile("s_mov_b32 m0, %1\n\ts_nop 0\n\tglobal_load_lds_dwordx4 %0, off" :: "v"(g), "s"(lds_off) : "memory");
}
typedef __attribute__((ext_vector_type(2))) float f32x2;
typedef __attribute__((ext_vector_type(2))) __bf16 bf16x2_t;
__device__ __forceinline__ unsigned cvtpk(f32x2 v) { return __builtin_bit_cast(unsigned, __builtin_convertvector(v, bf16x2_t)); }
__device__ __forceinline__ f32x2 unpk(unsigned u) { return (f32x2){__uint_as_float(u << 16), __uint_as_float(u & 0xffff0000u)}; }
__device__ __forceinline__ unsigned epi1(f32x2 a, f32x2 b) {
    f32x2 y = unpk(cvtpk(a)) + b;
    y = unpk(cvtpk(y));
    y = __builtin_elementwise_max(y, (f32x2){0.f, 0.f});
    return cvtpk(y * y);
}
__device__ __forceinline__ unsigned epi2(f32x2 a, f32x2 b) { return cvtpk(unpk(cvtpk(a)) + b); }
#define MFMA16(a, b, c) __builtin_amdgcn_mfma_f32_16x16x32_bf16(a, b, c, 0, 0, 0)

template <int NS>
__global__ __launch_bounds__(384, 1) void k1_gemm1(const u16* __restrict__ x, const u16* __restrict__ w1, const u16* __restrict__ b1,
                                                    const u16* __restrict__ w2, u16* __restrict__ hF, u16* __restrict__ w2F TSARG) {
    TS(0);
    __shared__ __attribute__((aligned(16))) unsigned char lds[NS * 32768];
    const int id = blockIdx.x, xcd = id & 7, j = id >> 3;
    const int wave = __builtin_amdgcn_readfirstlane(threadIdx.x >> 6), lane = threadIdx.x & 63;
    const int g = lane >> 4;

    if (j >= 28) {
        if (wave >= 4) return;
        const int half = xcd & 1;
        const int wi = (xcd >> 1) * 16 + (j - 28) * 4 + wave;
        u16 v[17][8];
#pragma unroll
        for (int u = 0; u < 17; ++u) {
            const int lf = wi + 64 * u;
            const int fi = half * 1080 + (lf < 1080 ? lf : 1079);
            const int db = fi / 270, rem = fi - db * 270, kt = rem / 5, di = rem - kt * 5;
            const u16* src = w2 + (size_t)(32 * kt + 4 * g) * D_ + 80 * db + 16 * di + (lane & 15);
#pragma unroll
            for (int jj = 0; jj < 8; ++jj) v[u][jj] = src[(size_t)(16 * (jj >> 2) + (jj & 3)) * D_];
        }
#pragma unroll
        for (int u = 0; u < 17; ++u) {
            const int lf = wi + 64 * u;
            if (lf < 1080) {
                const int fi = half * 1080 + lf;
                u32x4 o;
#pragma unroll
                for (int q = 0; q < 4; ++q) o[q] = (unsigned)v[u][2 * q] | ((unsigned)v[u][2 * q + 1] << 16);
                *reinterpret_cast<u32x4*>(w2F + (size_t)fi * 512 + lane * 8) = o;
            }
        }
        TS(1); TS(2);
        return;
    }

    const int mt = 4 * (xcd >> 1) + (j & 3);
    const int c = 7 * (xcd & 1) + (j >> 2);
    const int m0 = 128 * mt;
    const int f0 = c == 13 ? (F_ - 128) : 128 * c;

    if (wave >= 4) {
        const unsigned ldsbase = (unsigned)(size_t)(AS3 unsigned char*)lds;
        auto run_loader = [&](auto issue) {
#pragma unroll
            for (int kc = 0; kc < NS - 1; ++kc) issue(kc);
#pragma unroll
            for (int kc = 0; kc < 10; ++kc) {
                const int ahead = ((kc + NS - 2 < 9) ? (NS - 2) : (9 - kc));
                if (ahead >= 4) asm volatile("s_waitcnt vmcnt(63)" ::: "memory");
                else if (ahead == 3) asm volatile("s_waitcnt vmcnt(48)" ::: "memory");
                else if (ahead == 2) asm volatile("s_waitcnt vmcnt(32)" ::: "memory");
                else if (ahead == 1) asm volatile("s_waitcnt vmcnt(16)" ::: "memory");
                else asm volatile("s_waitcnt vmcnt(0)" ::: "memory");
                __builtin_amdgcn_s_barrier();
                if (kc + NS - 1 < 10) issue(kc + NS - 1);
            }
        };
        if (wave == 4) {
            const u16* px = x + (size_t)(m0 + (lane >> 3)) * D_ + 8 * ((lane & 7) ^ (lane >> 3));
            run_loader([&](int kc) {
                const unsigned sb = ldsbase + (kc % NS) * 32768;
#pragma unroll
                for (int i = 0; i < 16; ++i) dma16(px + (size_t)i * 8 * D_ + 64 * kc, sb + i * 1024);
            });
        } else {
            const int blk0 = ((lane & 15) >> 1) ^ (lane >> 4);
            const u16* pw0 = w1 + (size_t)(lane >> 4) * F_ + f0 + 16 * blk0 + 8 * (lane & 1);
            const u16* pw1 = w1 + (size_t)(lane >> 4) * F_ + f0 + 16 * (blk0 ^ 4) + 8 * (lane & 1);
            run_loader([&](int kc) {
                const unsigned sb = ldsbase + (kc % NS) * 32768;
#pragma unroll
                for (int i = 0; i < 16; ++i) dma16(((i & 2) ? pw1 : pw0) + (size_t)(64 * kc + 4 * i) * F_, sb + 16384 + i * 1024);
            });
        }
        TS(1); TS(2);
        return;
    }

    const int wm = wave >> 1, wf = wave & 1;
    unsigned tch[2];
#pragma unroll
    for (int i = 0; i < 2; ++i) {
        constexpr int SK = NS - 1, XL = 10 - SK, WR = 640 - 64 * SK;
        const int L = (j * 4 + wave) * 128 + i * 64 + lane;
        const u16* a;
        if (L < 512 * XL) a = x + (size_t)(512 * (xcd >> 1) + L / XL) * D_ + (SK + L % XL) * 64;
        else {
            const int L2 = L - 512 * XL < WR * 14 ? L - 512 * XL : 0;
            const int col = 896 * (xcd & 1) + (L2 % 14) * 64;
            a = w1 + (size_t)(64 * SK + L2 / 14) * F_ + (col < F_ ? col : 0);
        }
        tch[i] = *reinterpret_cast<const unsigned*>(a);
    }
    u32x2 bias[4];
#pragma unroll
    for (int t = 0; t < 4; ++t) bias[t] = *reinterpret_cast<const u32x2*>(b1 + f0 + 64 * wf + 16 * t + 4 * g);
#pragma unroll
    for (int t = 0; t < 4; ++t) asm volatile("" : "+v"(bias[t]));

    const int xrow = (64 * wm + (lane & 15)) * 128;
    const int xoff0 = xrow + ((g ^ (lane & 7)) * 16);
    const int xoff1 = xrow + (((4 + g) ^ (lane & 7)) * 16);
    const int q = (lane >> 2) & 3, p = lane & 3, sw = q | ((g & 1) << 2);
    const int wbase = 16384 + (8 * g + q) * 256 + 8 * p;
    int woff[4];
#pragma unroll
    for (int t = 0; t < 4; ++t) woff[t] = wbase + (((4 * wf + t) ^ sw) << 5);

    f32x4 acc[4][4];
#pragma unroll
    for (int t = 0; t < 4; ++t)
#pragma unroll
        for (int mi = 0; mi < 4; ++mi) acc[t][mi] = (f32x4){0.f, 0.f, 0.f, 0.f};

    bf16x8 xf[2][2][4], wfr[2][2][4];
#pragma unroll
    for (int kc = 0; kc <= 10; ++kc) {
        if (kc < 10) {
            __builtin_amdgcn_s_barrier();
            if (kc == 9) TS(3);
            const unsigned char* sb = lds + (kc % NS) * 32768;
#pragma unroll
            for (int ks = 0; ks < 2; ++ks) {
#pragma unroll
                for (int mi = 0; mi < 4; ++mi)
                    xf[kc & 1][ks][mi] = *reinterpret_cast<const bf16x8*>(sb + (ks ? xoff1 : xoff0) + mi * 2048);
#pragma unroll
                for (int t = 0; t < 4; ++t) {
                    bf16x4 lo = __builtin_amdgcn_ds_read_tr16_b64_v4i16((AS3 bf16x4*)(sb + woff[t] + ks * 8192));
                    bf16x4 hi = __builtin_amdgcn_ds_read_tr16_b64_v4i16((AS3 bf16x4*)(sb + woff[t] + ks * 8192 + 1024));
                    wfr[kc & 1][ks][t] = __builtin_shufflevector(lo, hi, 0, 1, 2, 3, 4, 5, 6, 7);
                }
            }
        }
        __builtin_amdgcn_sched_barrier(0);
        if (kc > 0) {
#pragma unroll
            for (int ks = 0; ks < 2; ++ks)
#pragma unroll
                for (int t = 0; t < 4; ++t)
#pragma unroll
                    for (int mi = 0; mi < 4; ++mi) acc[t][mi] = MFMA16(wfr[(kc - 1) & 1][ks][t], xf[(kc - 1) & 1][ks][mi], acc[t][mi]);
        }
        __builtin_amdgcn_sched_barrier(0);
        if (kc < 10) asm volatile("s_waitcnt lgkmcnt(0)" ::: "memory");
    }

    asm volatile("" :: "v"(tch[0]), "v"(tch[1]));
    TS(1);
    if (c == 13 && wf == 0) { TS(2); return; }
    const int mb = 2 * mt + wm;
#pragma unroll
    for (int u = 0; u < 2; ++u) {
        const int kt = (f0 + 64 * wf) / 32 + u;
#pragma unroll
        for (int mi = 0; mi < 4; ++mi) {
            u32x4 o;
#pragma unroll
            for (int hh = 0; hh < 2; ++hh) {
                const int t = 2 * u + hh;
                o[2 * hh] = epi1((f32x2){acc[t][mi][0], acc[t][mi][1]}, unpk(bias[t][0]));
                o[2 * hh + 1] = epi1((f32x2){acc[t][mi][2], acc[t][mi][3]}, unpk(bias[t][1]));
            }
            *reinterpret_cast<u32x4*>(hF + ((size_t)((mb * KT_ + kt) * 4 + mi) * 64 + lane) * 8) = o;
        }
    }
    TS(2);
}

template <int P>
__global__ __launch_bounds__(256, 1) void k2_gemm2(const u16* __restrict__ hF, const u16* __restrict__ w2F, const u16* __restrict__ b2,
                                                    u16* __restrict__ out TSARG) {
    TS(0);
    __shared__ __attribute__((aligned(16))) f32x4 red[4 * 4 * 5 * 64];
    const int id = blockIdx.x, xcd = id & 7, j = id >> 3;
    const int mb = 8 * (xcd >> 1) + (j & 7), db = 4 * (xcd & 1) + (j >> 3);
    const int wave = __builtin_amdgcn_readfirstlane(threadIdx.x >> 6), lane = threadIdx.x & 63;
    const int g = lane >> 4;
    const int kt0 = wave * 13 + (wave < 2 ? wave : 2);
    const int nk = wave < 2 ? 14 : 13;
    const u16* pa = hF + ((size_t)(mb * KT_ + kt0) * 4) * 512 + lane * 8;
    const u16* pb = w2F + ((size_t)(db * KT_ + kt0) * 5) * 512 + lane * 8;

    u32x2 bias[5];
#pragma unroll
    for (int di = 0; di < 5; ++di) bias[di] = *reinterpret_cast<const u32x2*>(b2 + 80 * db + 16 * di + 4 * g);

    f32x4 acc[5][4];
#pragma unroll
    for (int di = 0; di < 5; ++di)
#pragma unroll
        for (int mi = 0; mi < 4; ++mi) acc[di][mi] = (f32x4){0.f, 0.f, 0.f, 0.f};

    bf16x8 A[P][5], B[P][4];
    auto load = [&](int s, int slot) {
#pragma unroll
        for (int mi = 0; mi < 4; ++mi) B[slot][mi] = *reinterpret_cast<const bf16x8*>(pa + (size_t)s * 2048 + mi * 512);
#pragma unroll
        for (int di = 0; di < 5; ++di) A[slot][di] = *reinterpret_cast<const bf16x8*>(pb + (size_t)s * 2560 + di * 512);
    };
#pragma unroll
    for (int s = 0; s < P; ++s) load(s, s);
    __builtin_amdgcn_sched_barrier(0);
#pragma unroll
    for (int di = 0; di < 5; ++di) asm volatile("" : "+v"(bias[di]));
#pragma unroll
    for (int s = 0; s < 14; ++s) {
        if (s < 13 || nk == 14) {
#pragma unroll
            for (int di = 0; di < 5; ++di)
#pragma unroll
                for (int mi = 0; mi < 4; ++mi) acc[di][mi] = MFMA16(A[s % P][di], B[s % P][mi], acc[di][mi]);
        }
        if (s + P < 13 || (s + P == 13 && nk == 14)) load(s + P, s % P);
        __builtin_amdgcn_sched_barrier(0);
    }

    TS(1);
#pragma unroll
    for (int di = 0; di < 5; ++di)
#pragma unroll
        for (int mi = 0; mi < 4; ++mi) red[((wave * 4 + mi) * 5 + di) * 64 + lane] = acc[di][mi];
    TS(3);
    __syncthreads();
    TS(4);
    f32x4 fin[5];
#pragma unroll
    for (int di = 0; di < 5; ++di) {
        fin[di] = red[((0 * 4 + wave) * 5 + di) * 64 + lane];
#pragma unroll
        for (int sw_ = 1; sw_ < 4; ++sw_) fin[di] += red[((sw_ * 4 + wave) * 5 + di) * 64 + lane];
    }
    for (int di = 0; di < 5; ++di) asm volatile("" :: "v"(fin[di]));
    TS(5);
    const int m = 64 * mb + 16 * wave + (lane & 15);
#pragma unroll
    for (int di = 0; di < 5; ++di) {
        u32x2 o = {epi2((f32x2){fin[di][0], fin[di][1]}, unpk(bias[di][0])), epi2((f32x2){fin[di][2], fin[di][3]}, unpk(bias[di][1]))};
        *reinterpret_cast<u32x2*>(out + (size_t)m * D_ + 80 * db + 16 * di + 4 * g) = o;
    }
    TS(2);
}
}

extern "C" void kernel_launch(void* const* d_in, const int* in_sizes, int n_in, void* d_out, int out_size, void* d_ws, size_t ws_size,
                              hipStream_t stream) {
    const u16* x = (const u16*)d_in[0];
    const u16* w1 = (const u16*)d_in[1];
    const u16* b1 = (const u16*)d_in[2];
    const u16* w2 = (const u16*)d_in[3];
    const u16* b2 = (const u16*)d_in[4];
    u16* hF = (u16*)d_ws;
    u16* w2F = hF + (size_t)HF_ELEMS;
    k1_gemm1<K1_NS><<<256, 384, 0, stream>>>(x, w1, b1, w2, hF, w2F TSPASS(ts));
    k2_gemm2<6><<<256, 256, 0, stream>>>(hF, w2F, b2, (u16*)d_out TSPASS(ts + 8 * 4096));
}
```

```cpp
#include <hip/hip_runtime.h>
#include <hip/hip_bf16.h>
#include <stdint.h>

typedef unsigned short u16;
typedef __attribute__((ext_vector_type(8))) short bf16x8;
typedef __attribute__((ext_vector_type(4))) short bf16x4;
typedef __attribute__((ext_vector_type(4))) float f32x4;
typedef __attribute__((ext_vector_type(4))) unsigned u32x4;
typedef __attribute__((ext_vector_type(2))) unsigned u32x2;
#define TSARG
#define TSPASS(p)
#define TS(i)
#define AS1 __attribute__((address_space(1)))
#define AS3 __attribute__((address_space(3)))

#define K1_NS 4
#define K1_TOUCH 1
#define K2_TOUCH 0
namespace {
constexpr int S_ = 2048, D_ = 640, F_ = 1728;
constexpr int KT_ = 54;
constexpr int HF_ELEMS = S_ * F_;

__device__ __forceinline__ float bf2f(u16 u) { return __uint_as_float(((unsigned)u) << 16); }
__device__ __forceinline__ float bfround(float f) { return (float)(__bf16)f; }
__device__ __forceinline__ unsigned pack2(float lo, float hi) {
    __bf16 a = (__bf16)lo, b = (__bf16)hi;
    return (unsigned)__builtin_bit_cast(u16, a) | ((unsigned)__builtin_bit_cast(u16, b) << 16);
}
__device__ __forceinline__ void dma16(const void* g, unsigned lds_off) {
    asm volatile("s_mov_b32 m0, %1\n\ts_nop 0\n\tglobal_load_lds_dwordx4 %0, off" :: "v"(g), "s"(lds_off) : "memory");
}
typedef __attribute__((ext_vector_type(2))) float f32x2;
typedef __attribute__((ext_vector_type(2))) __bf16 bf16x2_t;
__device__ __forceinline__ unsigned cvtpk(f32x2 v) { return __builtin_bit_cast(unsigned, __builtin_convertvector(v, bf16x2_t)); }
__device__ __forceinline__ f32x2 unpk(unsigned u) { return (f32x2){__uint_as_float(u << 16), __uint_as_float(u & 0xffff0000u)}; }
__device__ __forceinline__ unsigned epi1(f32x2 a, f32x2 b) {
    f32x2 y = unpk(cvtpk(a)) + b;
    y = unpk(cvtpk(y));
    y = __builtin_elementwise_max(y, (f32x2){0.f, 0.f});
    return cvtpk(y * y);
}
__device__ __forceinline__ unsigned epi2(f32x2 a, f32x2 b) { return cvtpk(unpk(cvtpk(a)) + b); }
#define MFMA16(a, b, c) __builtin_amdgcn_mfma_f32_16x16x32_bf16(a, b, c, 0, 0, 0)

template <int NS>
__global__ __launch_bounds__(384, 1) void k1_gemm1(const u16* __restrict__ x, const u16* __restrict__ w1, const u16* __restrict__ b1,
                                                    const u16* __restrict__ w2, u16* __restrict__ hF, u16* __restrict__ w2F TSARG) {
    TS(0);
    __shared__ __attribute__((aligned(16))) unsigned char lds[NS * 32768];
    const int id = blockIdx.x, xcd = id & 7, j = id >> 3;
    const int wave = __builtin_amdgcn_readfirstlane(threadIdx.x >> 6), lane = threadIdx.x & 63;
    const int g = lane >> 4;

    if (j >= 28) {
        if (wave >= 4) return;
        const int half = xcd & 1;
        const int wi = (xcd >> 1) * 16 + (j - 28) * 4 + wave;
        u16 v[17][8];
#pragma unroll
        for (int u = 0; u < 17; ++u) {
            const int lf = wi + 64 * u;
            const int fi = half * 1080 + (lf < 1080 ? lf : 1079);
            const int db = fi / 270, rem = fi - db * 270, kt = rem / 5, di = rem - kt * 5;
            const u16* src = w2 + (size_t)(32 * kt + 4 * g) * D_ + 80 * db + 16 * di + (lane & 15);
#pragma unroll
            for (int jj = 0; jj < 8; ++jj) v[u][jj] = src[(size_t)(16 * (jj >> 2) + (jj & 3)) * D_];
        }
#pragma unroll
        for (int u = 0; u < 17; ++u) {
            const int lf = wi + 64 * u;
            if (lf < 1080) {
                const int fi = half * 1080 + lf;
                u32x4 o;
#pragma unroll
                for (int q = 0; q < 4; ++q) o[q] = (unsigned)v[u][2 * q] | ((unsigned)v[u][2 * q + 1] << 16);
                *reinterpret_cast<u32x4*>(w2F + (size_t)fi * 512 + lane * 8) = o;
            }
        }
        TS(1); TS(2);
        return;
    }

    const int mt = 4 * (xcd >> 1) + (j & 3);
    const int c = 7 * (xcd & 1) + (j >> 2);
    const int m0 = 128 * mt;
    const int f0 = c == 13 ? (F_ - 128) : 128 * c;

    if (wave >= 4) {
        const unsigned ldsbase = (unsigned)(size_t)(AS3 unsigned char*)lds;
        auto run_loader = [&](auto issue) {
#pragma unroll
            for (int kc = 0; kc < NS - 1; ++kc) issue(kc);
#pragma unroll
            for (int kc = 0; kc < 10; ++kc) {
                const int ahead = ((kc + NS - 2 < 9) ? (NS - 2) : (9 - kc));
                if (ahead >= 4) asm volatile("s_waitcnt vmcnt(63)" ::: "memory");
                else if (ahead == 3) asm volatile("s_waitcnt vmcnt(48)" ::: "memory");
                else if (ahead == 2) asm volatile("s_waitcnt vmcnt(32)" ::: "memory");
                else if (ahead == 1) asm volatile("s_waitcnt vmcnt(16)" ::: "memory");
                else asm volatile("s_waitcnt vmcnt(0)" ::: "memory");
                __builtin_amdgcn_s_barrier();
                if (kc + NS - 1 < 10) issue(kc + NS - 1);
            }
        };
        if (wave == 4) {
            const u16* px = x + (size_t)(m0 + (lane >> 3)) * D_ + 8 * ((lane & 7) ^ (lane >> 3));
            run_loader([&](int kc) {
                const unsigned sb = ldsbase + (kc % NS) * 32768;
#pragma unroll
                for (int i = 0; i < 16; ++i) dma16(px + (size_t)i * 8 * D_ + 64 * kc, sb + i * 1024);
            });
        } else {
            const int blk0 = ((lane & 15) >> 1) ^ (lane >> 4);
            const u16* pw0 = w1 + (size_t)(lane >> 4) * F_ + f0 + 16 * blk0 + 8 * (lane & 1);
            const u16* pw1 = w1 + (size_t)(lane >> 4) * F_ + f0 + 16 * (blk0 ^ 4) + 8 * (lane & 1);
            run_loader([&](int kc) {
                const unsigned sb = ldsbase + (kc % NS) * 32768;
#pragma unroll
                for (int i = 0; i < 16; ++i) dma16(((i & 2) ? pw1 : pw0) + (size_t)(64 * kc + 4 * i) * F_, sb + 16384 + i * 1024);
            });
        }
        TS(1); TS(2);
        return;
    }

    const int wm = wave >> 1, wf = wave & 1;
    unsigned tch[2];
#pragma unroll
    for (int i = 0; i < 2; ++i) {
        const int L = (j * 4 + wave) * 128 + i * 64 + lane;
        const u16* a;
        if (L < 5120) a = x + (size_t)(512 * (xcd >> 1) + L / 10) * D_ + (L % 10) * 64;
        else {
            const int L2 = L - 5120 < 8960 ? L - 5120 : 0;
            const int col = 896 * (xcd & 1) + (L2 % 14) * 64;
            a = w1 + (size_t)(L2 / 14) * F_ + (col < F_ ? col : 0);
        }
        tch[i] = *reinterpret_cast<const unsigned*>(a);
    }
    u32x2 bias[4];
#pragma unroll
    for (int t = 0; t < 4; ++t) bias[t] = *reinterpret_cast<const u32x2*>(b1 + f0 + 64 * wf + 16 * t + 4 * g);
#pragma unroll
    for (int t = 0; t < 4; ++t) asm volatile("" : "+v"(bias[t]));

    const int xrow = (64 * wm + (lane & 15)) * 128;
    const int xoff0 = xrow + ((g ^ (lane & 7)) * 16);
    const int xoff1 = xrow + (((4 + g) ^ (lane & 7)) * 16);
    const int q = (lane >> 2) & 3, p = lane & 3, sw = q | ((g & 1) << 2);
    const int wbase = 16384 + (8 * g + q) * 256 + 8 * p;
    int woff[4];
#pragma unroll
    for (int t = 0; t < 4; ++t) woff[t] = wbase + (((4 * wf + t) ^ sw) << 5);

    f32x4 acc[4][4];
#pragma unroll
    for (int t = 0; t < 4; ++t)
#pragma unroll
        for (int mi = 0; mi < 4; ++mi) acc[t][mi] = (f32x4){0.f, 0.f, 0.f, 0.f};

    bf16x8 xf[2][2][4], wfr[2][2][4];
#pragma unroll
    for (int kc = 0; kc <= 10; ++kc) {
        if (kc < 10) {
            __builtin_amdgcn_s_barrier();
            if (kc == 9) TS(3);
            const unsigned char* sb = lds + (kc % NS) * 32768;
#pragma unroll
            for (int ks = 0; ks < 2; ++ks) {
#pragma unroll
                for (int mi = 0; mi < 4; ++mi)
                    xf[kc & 1][ks][mi] = *reinterpret_cast<const bf16x8*>(sb + (ks ? xoff1 : xoff0) + mi * 2048);
#pragma unroll
                for (int t = 0; t < 4; ++t) {
                    bf16x4 lo = __builtin_amdgcn_ds_read_tr16_b64_v4i16((AS3 bf16x4*)(sb + woff[t] + ks * 8192));
                    bf16x4 hi = __builtin_amdgcn_ds_read_tr16_b64_v4i16((AS3 bf16x4*)(sb + woff[t] + ks * 8192 + 1024));
                    wfr[kc & 1][ks][t] = __builtin_shufflevector(lo, hi, 0, 1, 2, 3, 4, 5, 6, 7);
                }
            }
        }
        __builtin_amdgcn_sched_barrier(0);
        if (kc > 0) {
#pragma unroll
            for (int ks = 0; ks < 2; ++ks)
#pragma unroll
                for (int t = 0; t < 4; ++t)
#pragma unroll
                    for (int mi = 0; mi < 4; ++mi) acc[t][mi] = MFMA16(wfr[(kc - 1) & 1][ks][t], xf[(kc - 1) & 1][ks][mi], acc[t][mi]);
        }
        __builtin_amdgcn_sched_barrier(0);
        if (kc < 10) asm volatile("s_waitcnt lgkmcnt(0)" ::: "memory");
    }

    asm volatile("" :: "v"(tch[0]), "v"(tch[1]));
    TS(1);
    if (c == 13 && wf == 0) { TS(2); return; }
    const int mb = 2 * mt + wm;
#pragma unroll
    for (int u = 0; u < 2; ++u) {
        const int kt = (f0 + 64 * wf) / 32 + u;
#pragma unroll
        for (int mi = 0; mi < 4; ++mi) {
            u32x4 o;
#pragma unroll
            for (int hh = 0; hh < 2; ++hh) {
                const int t = 2 * u + hh;
                o[2 * hh] = epi1((f32x2){acc[t][mi][0], acc[t][mi][1]}, unpk(bias[t][0]));
                o[2 * hh + 1] = epi1((f32x2){acc[t][mi][2], acc[t][mi][3]}, unpk(bias[t][1]));
            }
            *reinterpret_cast<u32x4*>(hF + ((size_t)((mb * KT_ + kt) * 4 + mi) * 64 + lane) * 8) = o;
        }
    }
    TS(2);
}

template <int P>
__global__ __launch_bounds__(256, 1) void k2_gemm2(const u16* __restrict__ hF, const u16* __restrict__ w2F, const u16* __restrict__ b2,
                                                    u16* __restrict__ out TSARG) {
    TS(0);
    __shared__ __attribute__((aligned(16))) f32x4 red[4 * 4 * 5 * 64];
    const int id = blockIdx.x, xcd = id & 7, j = id >> 3;
    const int mb = 8 * (xcd >> 1) + (j & 7), db = 4 * (xcd & 1) + (j >> 3);
    const int wave = __builtin_amdgcn_readfirstlane(threadIdx.x >> 6), lane = threadIdx.x & 63;
    const int g = lane >> 4;
    const int kt0 = wave * 13 + (wave < 2 ? wave : 2);
    const int nk = wave < 2 ? 14 : 13;
    const u16* pa = hF + ((size_t)(mb * KT_ + kt0) * 4) * 512 + lane * 8;
    const u16* pb = w2F + ((size_t)(db * KT_ + kt0) * 5) * 512 + lane * 8;

    u32x2 bias[5];
#pragma unroll
    for (int di = 0; di < 5; ++di) bias[di] = *reinterpret_cast<const u32x2*>(b2 + 80 * db + 16 * di + 4 * g);

    f32x4 acc[5][4];
#pragma unroll
    for (int di = 0; di < 5; ++di)
#pragma unroll
        for (int mi = 0; mi < 4; ++mi) acc[di][mi] = (f32x4){0.f, 0.f, 0.f, 0.f};

    bf16x8 A[P][5], B[P][4];
    auto load = [&](int s, int slot) {
#pragma unroll
        for (int mi = 0; mi < 4; ++mi) B[slot][mi] = *reinterpret_cast<const bf16x8*>(pa + (size_t)s * 2048 + mi * 512);
#pragma unroll
        for (int di = 0; di < 5; ++di) A[slot][di] = *reinterpret_cast<const bf16x8*>(pb + (size_t)s * 2560 + di * 512);
    };
#pragma unroll
    for (int s = 0; s < P; ++s) load(s, s);
    __builtin_amdgcn_sched_barrier(0);
#pragma unroll
    for (int di = 0; di < 5; ++di) asm volatile("" : "+v"(bias[di]));
#pragma unroll
    for (int s = 0; s < 14; ++s) {
        if (s < 13 || nk == 14) {
#pragma unroll
            for (int di = 0; di < 5; ++di)
#pragma unroll
                for (int mi = 0; mi < 4; ++mi) acc[di][mi] = MFMA16(A[s % P][di], B[s % P][mi], acc[di][mi]);
        }
        if (s + P < 13 || (s + P == 13 && nk == 14)) load(s + P, s % P);
        __builtin_amdgcn_sched_barrier(0);
    }

    TS(1);
#pragma unroll
    for (int di = 0; di < 5; ++di)
#pragma unroll
        for (int mi = 0; mi < 4; ++mi) red[((wave * 4 + mi) * 5 + di) * 64 + lane] = acc[di][mi];
    TS(3);
    __syncthreads();
    TS(4);
    f32x4 fin[5];
#pragma unroll
    for (int di = 0; di < 5; ++di) {
        fin[di] = red[((0 * 4 + wave) * 5 + di) * 64 + lane];
#pragma unroll
        for (int sw_ = 1; sw_ < 4; ++sw_) fin[di] += red[((sw_ * 4 + wave) * 5 + di) * 64 + lane];
    }
    for (int di = 0; di < 5; ++di) asm volatile("" :: "v"(fin[di]));
    TS(5);
    const int m = 64 * mb + 16 * wave + (lane & 15);
#pragma unroll
    for (int di = 0; di < 5; ++di) {
        u32x2 o = {epi2((f32x2){fin[di][0], fin[di][1]}, unpk(bias[di][0])), epi2((f32x2){fin[di][2], fin[di][3]}, unpk(bias[di][1]))};
        *reinterpret_cast<u32x2*>(out + (size_t)m * D_ + 80 * db + 16 * di + 4 * g) = o;
    }
    TS(2);
}
}

extern "C" void kernel_launch(void* const* d_in, const int* in_sizes, int n_in, void* d_out, int out_size, void* d_ws, size_t ws_size,
                              hipStream_t stream) {
    const u16* x = (const u16*)d_in[0];
    const u16* w1 = (const u16*)d_in[1];
    const u16* b1 = (const u16*)d_in[2];
    const u16* w2 = (const u16*)d_in[3];
    const u16* b2 = (const u16*)d_in[4];
    u16* hF = (u16*)d_ws;
    u16* w2F = hF + (size_t)HF_ELEMS;
    k1_gemm1<K1_NS><<<256, 384, 0, stream>>>(x, w1, b1, w2, hF, w2F TSPASS(ts));
    k2_gemm2<6><<<256, 256, 0, stream>>>(hF, w2F, b2, (u16*)d_out TSPASS(ts + 8 * 4096));
}
```

```cpp
#include <hip/hip_runtime.h>
#include <hip/hip_bf16.h>
#include <stdint.h>

typedef unsigned short u16;
typedef __attribute__((ext_vector_type(8))) short bf16x8;
typedef __attribute__((ext_vector_type(4))) short bf16x4;
typedef __attribute__((ext_vector_type(4))) float f32x4;
typedef __attribute__((ext_vector_type(4))) unsigned u32x4;
typedef __attribute__((ext_vector_type(2))) unsigned u32x2;
#define TSARG
#define TSPASS(p)
#define TS(i)
#define AS1 __attribute__((address_space(1)))
#define AS3 __attribute__((address_space(3)))

#define K1_NS 4
#define K2_P 3
#define K2_NW 8
#define K1_TOUCH 1
#define K2_TOUCH 0
namespace {
constexpr int S_ = 2048, D_ = 640, F_ = 1728;
constexpr int KT_ = 54;
constexpr int HF_ELEMS = S_ * F_;

__device__ __forceinline__ float bf2f(u16 u) { return __uint_as_float(((unsigned)u) << 16); }
__device__ __forceinline__ float bfround(float f) { return (float)(__bf16)f; }
__device__ __forceinline__ unsigned pack2(float lo, float hi) {
    __bf16 a = (__bf16)lo, b = (__bf16)hi;
    return (unsigned)__builtin_bit_cast(u16, a) | ((unsigned)__builtin_bit_cast(u16, b) << 16);
}
__device__ __forceinline__ void dma16(const void* g, unsigned lds_off) {
    asm volatile("s_mov_b32 m0, %1\n\ts_nop 0\n\tglobal_load_lds_dwordx4 %0, off" :: "v"(g), "s"(lds_off) : "memory");
}
typedef __attribute__((ext_vector_type(2))) float f32x2;
typedef __attribute__((ext_vector_type(2))) __bf16 bf16x2_t;
__device__ __forceinline__ unsigned cvtpk(f32x2 v) { return __builtin_bit_cast(unsigned, __builtin_convertvector(v, bf16x2_t)); }
__device__ __forceinline__ f32x2 unpk(unsigned u) { return (f32x2){__uint_as_float(u << 16), __uint_as_float(u & 0xffff0000u)}; }
__device__ __forceinline__ unsigned epi1(f32x2 a, f32x2 b) {
    f32x2 y = unpk(cvtpk(a)) + b;
    y = unpk(cvtpk(y));
    y = __builtin_elementwise_max(y, (f32x2){0.f, 0.f});
    return cvtpk(y * y);
}
__device__ __forceinline__ unsigned epi2(f32x2 a, f32x2 b) { return cvtpk(unpk(cvtpk(a)) + b); }
#define MFMA16(a, b, c) __builtin_amdgcn_mfma_f32_16x16x32_bf16(a, b, c, 0, 0, 0)

template <int NS>
__global__ __launch_bounds__(384, 1) void k1_gemm1(const u16* __restrict__ x, const u16* __restrict__ w1, const u16* __restrict__ b1,
                                                    const u16* __restrict__ w2, u16* __restrict__ hF, u16* __restrict__ w2F TSARG) {
    TS(0);
    __shared__ __attribute__((aligned(16))) unsigned char lds[NS * 32768];
    const int id = blockIdx.x, xcd = id & 7, j = id >> 3;
    const int wave = __builtin_amdgcn_readfirstlane(threadIdx.x >> 6), lane = threadIdx.x & 63;
    const int g = lane >> 4;

    if (j >= 28) {
        if (wave >= 4) return;
        const int half = xcd & 1;
        const int wi = (xcd >> 1) * 16 + (j - 28) * 4 + wave;
        u16 v[17][8];
#pragma unroll
        for (int u = 0; u < 17; ++u) {
            const int lf = wi + 64 * u;
            const int fi = half * 1080 + (lf < 1080 ? lf : 1079);
            const int db = fi / 270, rem = fi - db * 270, kt = rem / 5, di = rem - kt * 5;
            const u16* src = w2 + (size_t)(32 * kt + 4 * g) * D_ + 80 * db + 16 * di + (lane & 15);
#pragma unroll
            for (int jj = 0; jj < 8; ++jj) v[u][jj] = src[(size_t)(16 * (jj >> 2) + (jj & 3)) * D_];
        }
#pragma unroll
        for (int u = 0; u < 17; ++u) {
            const int lf = wi + 64 * u;
            if (lf < 1080) {
                const int fi = half * 1080 + lf;
                u32x4 o;
#pragma unroll
                for (int q = 0; q < 4; ++q) o[q] = (unsigned)v[u][2 * q] | ((unsigned)v[u][2 * q + 1] << 16);
                *reinterpret_cast<u32x4*>(w2F + (size_t)fi * 512 + lane * 8) = o;
            }
        }
        TS(1); TS(2);
        return;
    }

    const int mt = 4 * (xcd >> 1) + (j & 3);
    const int c = 7 * (xcd & 1) + (j >> 2);
    const int m0 = 128 * mt;
    const int f0 = c == 13 ? (F_ - 128) : 128 * c;

    if (wave >= 4) {
        const unsigned ldsbase = (unsigned)(size_t)(AS3 unsigned char*)lds;
        auto run_loader = [&](auto issue) {
#pragma unroll
            for (int kc = 0; kc < NS - 1; ++kc) issue(kc);
#pragma unroll
            for (int kc = 0; kc < 10; ++kc) {
                const int ahead = ((kc + NS - 2 < 9) ? (NS - 2) : (9 - kc));
                if (ahead >= 4) asm volatile("s_waitcnt vmcnt(63)" ::: "memory");
                else if (ahead == 3) asm volatile("s_waitcnt vmcnt(48)" ::: "memory");
                else if (ahead == 2) asm volatile("s_waitcnt vmcnt(32)" ::: "memory");
                else if (ahead == 1) asm volatile("s_waitcnt vmcnt(16)" ::: "memory");
                else asm volatile("s_waitcnt vmcnt(0)" ::: "memory");
                __builtin_amdgcn_s_barrier();
                if (kc + NS - 1 < 10) issue(kc + NS - 1);
            }
        };
        if (wave == 4) {
            const u16* px = x + (size_t)(m0 + (lane >> 3)) * D_ + 8 * ((lane & 7) ^ (lane >> 3));
            run_loader([&](int kc) {
                const unsigned sb = ldsbase + (kc % NS) * 32768;
#pragma unroll
                for (int i = 0; i < 16; ++i) dma16(px + (size_t)i * 8 * D_ + 64 * kc, sb + i * 1024);
            });
        } else {
            const int blk0 = ((lane & 15) >> 1) ^ (lane >> 4);
            const u16* pw0 = w1 + (size_t)(lane >> 4) * F_ + f0 + 16 * blk0 + 8 * (lane & 1);
            const u16* pw1 = w1 + (size_t)(lane >> 4) * F_ + f0 + 16 * (blk0 ^ 4) + 8 * (lane & 1);
            run_loader([&](int kc) {
                const unsigned sb = ldsbase + (kc % NS) * 32768;
#pragma unroll
                for (int i = 0; i < 16; ++i) dma16(((i & 2) ? pw1 : pw0) + (size_t)(64 * kc + 4 * i) * F_, sb + 16384 + i * 1024);
            });
        }
        TS(1); TS(2);
        return;
    }

    const int wm = wave >> 1, wf = wave & 1;
    unsigned tch[2];
#pragma unroll
    for (int i = 0; i < 2; ++i) {
        const int L = (j * 4 + wave) * 128 + i * 64 + lane;
        const u16* a;
        if (L < 5120) a = x + (size_t)(512 * (xcd >> 1) + L / 10) * D_ + (L % 10) * 64;
        else {
            const int L2 = L - 5120 < 8960 ? L - 5120 : 0;
            const int col = 896 * (xcd & 1) + (L2 % 14) * 64;
            a = w1 + (size_t)(L2 / 14) * F_ + (col < F_ ? col : 0);
        }
        tch[i] = *reinterpret_cast<const unsigned*>(a);
    }
    u32x2 bias[4];
#pragma unroll
    for (int t = 0; t < 4; ++t) bias[t] = *reinterpret_cast<const u32x2*>(b1 + f0 + 64 * wf + 16 * t + 4 * g);
#pragma unroll
    for (int t = 0; t < 4; ++t) asm volatile("" : "+v"(bias[t]));

    const int xrow = (64 * wm + (lane & 15)) * 128;
    const int xoff0 = xrow + ((g ^ (lane & 7)) * 16);
    const int xoff1 = xrow + (((4 + g) ^ (lane & 7)) * 16);
    const int q = (lane >> 2) & 3, p = lane & 3, sw = q | ((g & 1) << 2);
    const int wbase = 16384 + (8 * g + q) * 256 + 8 * p;
    int woff[4];
#pragma unroll
    for (int t = 0; t < 4; ++t) woff[t] = wbase + (((4 * wf + t) ^ sw) << 5);

    f32x4 acc[4][4];
#pragma unroll
    for (int t = 0; t < 4; ++t)
#pragma unroll
        for (int mi = 0; mi < 4; ++mi) acc[t][mi] = (f32x4){0.f, 0.f, 0.f, 0.f};

    bf16x8 xf[2][2][4], wfr[2][2][4];
#pragma unroll
    for (int kc = 0; kc <= 10; ++kc) {
        if (kc < 10) {
            __builtin_amdgcn_s_barrier();
            if (kc == 9) TS(3);
            const unsigned char* sb = lds + (kc % NS) * 32768;
#pragma unroll
            for (int ks = 0; ks < 2; ++ks) {
#pragma unroll
                for (int mi = 0; mi < 4; ++mi)
                    xf[kc & 1][ks][mi] = *reinterpret_cast<const bf16x8*>(sb + (ks ? xoff1 : xoff0) + mi * 2048);
#pragma unroll
                for (int t = 0; t < 4; ++t) {
                    bf16x4 lo = __builtin_amdgcn_ds_read_tr16_b64_v4i16((AS3 bf16x4*)(sb + woff[t] + ks * 8192));
                    bf16x4 hi = __builtin_amdgcn_ds_read_tr16_b64_v4i16((AS3 bf16x4*)(sb + woff[t] + ks * 8192 + 1024));
                    wfr[kc & 1][ks][t] = __builtin_shufflevector(lo, hi, 0, 1, 2, 3, 4, 5, 6, 7);
                }
            }
        }
        __builtin_amdgcn_sched_barrier(0);
        if (kc > 0) {
#pragma unroll
            for (int ks = 0; ks < 2; ++ks)
#pragma unroll
                for (int t = 0; t < 4; ++t)
#pragma unroll
                    for (int mi = 0; mi < 4; ++mi) acc[t][mi] = MFMA16(wfr[(kc - 1) & 1][ks][t], xf[(kc - 1) & 1][ks][mi], acc[t][mi]);
        }
        __builtin_amdgcn_sched_barrier(0);
        if (kc < 10) asm volatile("s_waitcnt lgkmcnt(0)" ::: "memory");
    }

    asm volatile("" :: "v"(tch[0]), "v"(tch[1]));
    TS(1);
    if (c == 13 && wf == 0) { TS(2); return; }
    const int mb = 2 * mt + wm;
#pragma unroll
    for (int u = 0; u < 2; ++u) {
        const int kt = (f0 + 64 * wf) / 32 + u;
#pragma unroll
        for (int mi = 0; mi < 4; ++mi) {
            u32x4 o;
#pragma unroll
            for (int hh = 0; hh < 2; ++hh) {
                const int t = 2 * u + hh;
                o[2 * hh] = epi1((f32x2){acc[t][mi][0], acc[t][mi][1]}, unpk(bias[t][0]));
                o[2 * hh + 1] = epi1((f32x2){acc[t][mi][2], acc[t][mi][3]}, unpk(bias[t][1]));
            }
            *reinterpret_cast<u32x4*>(hF + ((size_t)((mb * KT_ + kt) * 4 + mi) * 64 + lane) * 8) = o;
        }
    }
    TS(2);
}

template <int P, int NW>
__global__ __launch_bounds__(NW * 64, 1) void k2_gemm2(const u16* __restrict__ hF, const u16* __restrict__ w2F, const u16* __restrict__ b2,
                                                    u16* __restrict__ out TSARG) {
    TS(0);
    __shared__ __attribute__((aligned(16))) f32x4 red[NW * 4 * 5 * 64];
    const int id = blockIdx.x, xcd = id & 7, j = id >> 3;
    const int mb = 8 * (xcd >> 1) + (j & 7), db = 4 * (xcd & 1) + (j >> 3);
    const int wave = __builtin_amdgcn_readfirstlane(threadIdx.x >> 6), lane = threadIdx.x & 63;
    const int g = lane >> 4;
    constexpr int NB = KT_ / NW, NR = KT_ - NB * NW;
    const int kt0 = wave * NB + (wave < NR ? wave : NR);
    const int nk = wave < NR ? NB + 1 : NB;
    const u16* pa = hF + ((size_t)(mb * KT_ + kt0) * 4) * 512 + lane * 8;
    const u16* pb = w2F + ((size_t)(db * KT_ + kt0) * 5) * 512 + lane * 8;

    u32x2 bias[5];
#pragma unroll
    for (int di = 0; di < 5; ++di) bias[di] = *reinterpret_cast<const u32x2*>(b2 + 80 * db + 16 * di + 4 * g);

    f32x4 acc[5][4];
#pragma unroll
    for (int di = 0; di < 5; ++di)
#pragma unroll
        for (int mi = 0; mi < 4; ++mi) acc[di][mi] = (f32x4){0.f, 0.f, 0.f, 0.f};

    bf16x8 A[P][5], B[P][4];
    auto load = [&](int s, int slot) {
#pragma unroll
        for (int mi = 0; mi < 4; ++mi) B[slot][mi] = *reinterpret_cast<const bf16x8*>(pa + (size_t)s * 2048 + mi * 512);
#pragma unroll
        for (int di = 0; di < 5; ++di) A[slot][di] = *reinterpret_cast<const bf16x8*>(pb + (size_t)s * 2560 + di * 512);
    };
#pragma unroll
    for (int s = 0; s < P; ++s) load(s, s);
    __builtin_amdgcn_sched_barrier(0);
#pragma unroll
    for (int di = 0; di < 5; ++di) asm volatile("" : "+v"(bias[di]));
#pragma unroll
    for (int s = 0; s < NB + 1; ++s) {
        if (s < NB || nk == NB + 1) {
#pragma unroll
            for (int di = 0; di < 5; ++di)
#pragma unroll
                for (int mi = 0; mi < 4; ++mi) acc[di][mi] = MFMA16(A[s % P][di], B[s % P][mi], acc[di][mi]);
        }
        if (s + P < NB || (s + P == NB && nk == NB + 1)) load(s + P, s % P);
        __builtin_amdgcn_sched_barrier(0);
    }

    TS(1);
#pragma unroll
    for (int di = 0; di < 5; ++di)
#pragma unroll
        for (int mi = 0; mi < 4; ++mi) red[((wave * 4 + mi) * 5 + di) * 64 + lane] = acc[di][mi];
    TS(3);
    __syncthreads();
    TS(4);
    const int fmi = NW == 4 ? wave : wave >> 1;
    const int m = 64 * mb + 16 * fmi + (lane & 15);
#pragma unroll
    for (int di = 0; di < 5; ++di) {
        if (NW == 4 || ((wave & 1) ? di >= 3 : di < 3)) {
            f32x4 fin = red[((0 * 4 + fmi) * 5 + di) * 64 + lane];
#pragma unroll
            for (int sw_ = 1; sw_ < NW; ++sw_) fin += red[((sw_ * 4 + fmi) * 5 + di) * 64 + lane];
            u32x2 o = {epi2((f32x2){fin[0], fin[1]}, unpk(bias[di][0])), epi2((f32x2){fin[2], fin[3]}, unpk(bias[di][1]))};
            *reinterpret_cast<u32x2*>(out + (size_t)m * D_ + 80 * db + 16 * di + 4 * g) = o;
        }
    }
    TS(2);
}
}

extern "C" void kernel_launch(void* const* d_in, const int* in_sizes, int n_in, void* d_out, int out_size, void* d_ws, size_t ws_size,
                              hipStream_t stream) {
    const u16* x = (const u16*)d_in[0];
    const u16* w1 = (const u16*)d_in[1];
    const u16* b1 = (const u16*)d_in[2];
    const u16* w2 = (const u16*)d_in[3];
    const u16* b2 = (const u16*)d_in[4];
    u16* hF = (u16*)d_ws;
    u16* w2F = hF + (size_t)HF_ELEMS;
    k1_gemm1<K1_NS><<<256, 384, 0, stream>>>(x, w1, b1, w2, hF, w2F TSPASS(ts));
    k2_gemm2<K2_P, K2_NW><<<256, K2_NW * 64, 0, stream>>>(hF, w2F, b2, (u16*)d_out TSPASS(ts + 8 * 4096));
}
```
